```python
import torch

def _softmax(x, axis=-1):
    return torch.softmax(x, dim=axis)
def _gelu(x):
    return torch.nn.functional.gelu(x, approximate="tanh")
def _silu(x):
    return torch.nn.functional.silu(x)
def _sigmoid(x):
    return torch.sigmoid(x)
def _layernorm(x, axis=-1, eps=1e-5):
    m = x.mean(dim=axis, keepdim=True)
    v = x.var(dim=axis, keepdim=True, unbiased=False)
    return (x - m) / torch.sqrt(v + eps)
def _scatter_add(tgt, idx, src, axis=0):
    return tgt.clone().index_add_(axis, idx, src)
def _segment_sum(src, seg, n):
    out = torch.zeros((n,) + src.shape[1:], dtype=src.dtype)
    return out.index_add_(0, seg, src)
def _pad(x, pw):
    flat = tuple(v for pair in reversed(pw) for v in pair)
    return torch.nn.functional.pad(x, flat)

kernel_name = 'concat_heads__mha_block_cached__sliding_block_cached__decode'

def setup_inputs():

    _w = (lambda a: (lambda t: t.to(torch.bfloat16) if t.is_floating_point() else t)(torch.as_tensor(a)))
    def _A_setup():
        B, Sq, D = 2, 1, 640
        H, Sk, Dh = 32, 4096, 192
        return {
            "x": _w((lambda *s: torch.randn(*s, dtype=torch.bfloat16) * 0.4)(B, Sq, D)),
            "k_cache": _w((lambda *s: torch.randn(*s, dtype=torch.bfloat16) * 3.2)(B, H, Sk, Dh)),
            "v_cache": _w((lambda *s: torch.randn(*s, dtype=torch.bfloat16) * 0.8)(B, H, Sk, Dh)),
            "wq": _w((lambda *s: torch.randn(*s, dtype=torch.bfloat16) * 3.2)(D, H * Dh) * (D ** -0.5)),
            "wo": _w((lambda *s: torch.randn(*s, dtype=torch.bfloat16) * 0.8)(H * Dh, D) * ((H * Dh) ** -0.5)),
        }
    _A_in = _A_setup()

    def _B_setup():
        B, Sq, D = 2, 1, 640
        Hq, Hkv, Sk, Dh = 32, 32, 4096, 192
        assert Hq % Hkv == 0, (Hq, Hkv)
        return {
            "x": _w((lambda *s: torch.randn(*s, dtype=torch.bfloat16) * 0.2)(B, Sq, D)),
            "k_cache": _w((lambda *s: torch.randn(*s, dtype=torch.bfloat16) * 3.2)(B, Hkv, Sk, Dh)),
            "v_cache": _w((lambda *s: torch.randn(*s, dtype=torch.bfloat16) * 1.6)(B, Hkv, Sk, Dh)),
            "wq": _w((lambda *s: torch.randn(*s, dtype=torch.bfloat16) * 3.2)(D, Hq * Dh) * (D ** -0.5)),
            "wo": _w((lambda *s: torch.randn(*s, dtype=torch.bfloat16) * 1.6)(Hq * Dh, D) * ((Hq * Dh) ** -0.5)),
        }
    _B_in = _B_setup()

    return {
        "A_x": _A_in["x"],
        "A_k_cache": _A_in["k_cache"],
        "A_v_cache": _A_in["v_cache"],
        "A_wq": _A_in["wq"],
        "A_wo": _A_in["wo"],
        "B_x": _B_in["x"],
        "B_k_cache": _B_in["k_cache"],
        "B_v_cache": _B_in["v_cache"],
        "B_wq": _B_in["wq"],
        "B_wo": _B_in["wo"],
    }

def reference(A_x, A_k_cache, A_v_cache, A_wq, A_wo, B_x, B_k_cache, B_v_cache, B_wq, B_wo):
    def _A_ref(x, k_cache, v_cache, wq, wo):
        B, Sq, D = x.shape
        H, Dh = 32, 192
        q = (lambda x, *d: x.permute(*d))((x @ wq).reshape(B, Sq, H, Dh), 0, 2, 1, 3)
        scores = torch.einsum("bhqd,bhkd->bhqk", q, k_cache) * (Dh ** -0.5)
        p = _softmax(scores, axis=-1)
        o = torch.einsum("bhqk,bhkd->bhqd", p, v_cache)
        return (lambda x, *d: x.permute(*d))(o, 0, 2, 1, 3).reshape(B, Sq, H * Dh) @ wo

    def _B_ref(x, k_cache, v_cache, wq, wo):
        B, Sq, D = x.shape
        Hq, Dh = 32, 192
        Hkv, Sk = k_cache.shape[1], k_cache.shape[2]
        W = min(4096, max(256, Sk // 4))
        rep = Hq // Hkv
        q = (lambda x, *d: x.permute(*d))((x @ wq).reshape(B, Sq, Hq, Dh), 0, 2, 1, 3)
        k = (lambda x, n, axis: torch.repeat_interleave(x, n, dim=axis))(k_cache[:, :, Sk - W:, :], rep, axis=1)
        v = (lambda x, n, axis: torch.repeat_interleave(x, n, dim=axis))(v_cache[:, :, Sk - W:, :], rep, axis=1)
        scores = torch.einsum("bhqd,bhkd->bhqk", q, k) * (Dh ** -0.5)
        p = _softmax(scores, axis=-1)
        o = torch.einsum("bhqk,bhkd->bhqd", p, v)
        return (lambda x, *d: x.permute(*d))(o, 0, 2, 1, 3).reshape(B, Sq, Hq * Dh) @ wo

    a = _A_ref(A_x, A_k_cache, A_v_cache, A_wq, A_wo)
    b = _B_ref(B_x, B_k_cache, B_v_cache, B_wq, B_wo)
    return torch.concatenate([a, b], axis=-1)
```

```cpp
#include <hip/hip_runtime.h>
#include <hip/hip_bf16.h>
#include <stdint.h>
#include <utility>

#define GAS __attribute__((address_space(1)))
typedef unsigned u32;
typedef unsigned long long u64;
typedef u32 u32x4 __attribute__((ext_vector_type(4)));
typedef float f32x4 __attribute__((ext_vector_type(4)));
typedef __bf16 bf16x2 __attribute__((ext_vector_type(2)));
typedef __bf16 bf16x8 __attribute__((ext_vector_type(8)));
typedef GAS u64 gu64;
#define RLX_AGENT __ATOMIC_RELAXED, __HIP_MEMORY_SCOPE_AGENT

#define NB 10

constexpr int DM = 640, NH = 32, SK = 4096, DH = 192, WIN = 1024;
constexpr int NWG = 256, NTHR = 512;
constexpr float C2 = 0.07216878364870322f * 1.4426950408889634f;
constexpr unsigned SPIN_CAP = 1u << 17;

constexpr int G1S = 208;
constexpr size_t WS_G1 = 4096, WS_G2 = WS_G1 + (size_t)NH * 2 * 8 * G1S * 8;

struct Args {
  const char* x[2]; const char* k[2]; const char* v[2]; const char* wq[2]; const char* wo[2];
  char* out; char* ws;
};

enum { T_WA, T_WB, T_KA, T_VA, T_KB, T_VB, T_WO, T_NONE };
constexpr int NITEM = 60;
constexpr int item_type(int n) {
  if (n < 10) return T_WA;
  if (n < 30) return ((n - 10) & 1) ? T_WB : T_KA;
  if (n < 36) return T_KA;
  if (n < 52) return T_VA;
  if (n < 56) return T_KB;
  if (n < 60) return T_VB;
  if (n < 63) return T_WO;
  return T_NONE;
}
constexpr int item_idx(int n) {
  if (n < 10) return n;
  if (n < 30) return (n - 10) >> 1;
  if (n < 36) return n - 20;
  if (n < 52) return n - 36;
  if (n < 56) return n - 52;
  if (n < 60) return n - 56;
  return n - 60;
}

template <class F, int... I> __device__ __forceinline__ void static_for_impl(F&& f, std::integer_sequence<int, I...>) {
  (f(std::integral_constant<int, I>{}), ...);
}
template <int N, class F> __device__ __forceinline__ void static_for(F&& f) { static_for_impl(f, std::make_integer_sequence<int, N>{}); }

__device__ __forceinline__ float dot2(u32 a, u32 b, float c) {
  return __builtin_amdgcn_fdot2_f32_bf16(__builtin_bit_cast(bf16x2, a), __builtin_bit_cast(bf16x2, b), c, false);
}
template <int CTRL> __device__ __forceinline__ float dpp(float x) {
  return __builtin_bit_cast(float, __builtin_amdgcn_mov_dpp(__builtin_bit_cast(int, x), CTRL, 0xf, 0xf, true));
}
constexpr int XOR1 = 0xB1, XOR2 = 0x4E, HMIR = 0x141, ROR8 = 0x128;
__device__ __forceinline__ float xoct_max(float x) {
  x = fmaxf(x, dpp<ROR8>(x));
  auto s = __builtin_amdgcn_permlane16_swap(__float_as_uint(x), __float_as_uint(x), false, false);
  x = fmaxf(__uint_as_float(s[0]), __uint_as_float(s[1]));
  auto t = __builtin_amdgcn_permlane32_swap(__float_as_uint(x), __float_as_uint(x), false, false);
  return fmaxf(__uint_as_float(t[0]), __uint_as_float(t[1]));
}
__device__ __forceinline__ float xoct_sum(float x) {
  x = x + dpp<ROR8>(x);
  auto s = __builtin_amdgcn_permlane16_swap(__float_as_uint(x), __float_as_uint(x), false, false);
  x = __uint_as_float(s[0]) + __uint_as_float(s[1]);
  auto t = __builtin_amdgcn_permlane32_swap(__float_as_uint(x), __float_as_uint(x), false, false);
  return __uint_as_float(t[0]) + __uint_as_float(t[1]);
}
__device__ __forceinline__ u32 pk_bf16(float lo, float hi) {
  bf16x2 r; r[0] = (__bf16)lo; r[1] = (__bf16)hi; return __builtin_bit_cast(u32, r);
}
typedef __amdgpu_buffer_rsrc_t rsrc_t;
__device__ __forceinline__ rsrc_t mk_rsrc(const char* p, u32 bytes) { return __builtin_amdgcn_make_buffer_rsrc((void*)p, 0, bytes, 0x00020000); }
template <int AUX> __device__ __forceinline__ u32x4 ldb(rsrc_t rs, u32 voff, u32 soff) { return __builtin_amdgcn_raw_buffer_load_b128(rs, voff, soff, AUX); }
constexpr int AUX_NT = 2, AUX_DF = 0;
__device__ __forceinline__ u64 gran(u32 tag_hi, u32 tag_lo, float v) {
  return ((u64)tag_hi << 32) | (u64)(tag_lo | ((__float_as_uint(v) + 0x80u) >> 8));
}

constexpr int L_QP = 0;
constexpr int L_OP = L_QP + 2 * 16 * DH;
constexpr int L_ML = L_OP + 2 * 16 * DH;
constexpr int L_Q = L_ML + 32;
constexpr int L_E = L_Q + 2 * 96;
constexpr int L_Y = L_E + 4;
constexpr int L_END = L_Y + 24 * 320;

__global__ void __launch_bounds__(NTHR, 2) fused_decode(Args a) {
  __shared__ __attribute__((aligned(16))) float lds[L_END];
  const int tid = threadIdx.x, lane = tid & 63;
  const int w = __builtin_amdgcn_readfirstlane(tid >> 6);
  const int wg = blockIdx.x, slot = wg >> 3;
  const int h = (wg & 7) * 4 + (slot >> 3), j = slot & 7, b = j >> 2, qtr = j & 3;
  const int r = lane >> 3, c = lane & 7;

  u64 epoch = 0;
  if (tid == 0) epoch = __hip_atomic_load((gu64*)a.ws, RLX_AGENT);

  u32 xa[5], xb[5];
  {
    const GAS u32* pa = (const GAS u32*)(a.x[0] + b * (DM * 2) + 160 * w + 20 * r);
    const GAS u32* pb = (const GAS u32*)(a.x[1] + b * (DM * 2) + 160 * w + 20 * r);
#pragma unroll
    for (int i = 0; i < 5; ++i) xa[i] = pa[i];
#pragma unroll
    for (int i = 0; i < 5; ++i) xb[i] = pb[i];
  }
  const u32 kv_off = r * 384 + c * 16;
  const u32 w_off = r * (10 * 12288) + c * 16;
  const size_t segA = ((size_t)(b * NH + h) * SK + qtr * 1024) * 384 + w * 3072;
  const size_t segB = ((size_t)(b * NH + h) * SK + (SK - WIN) + qtr * 256) * 384 + w * 3072;
  const size_t wqo = (size_t)w * (80 * 12288) + h * 384;
  const rsrc_t pKA = mk_rsrc(a.k[0] + segA, 15 * 24576 + 3072), pVA = mk_rsrc(a.v[0] + segA, 15 * 24576 + 3072);
  const rsrc_t pKB = mk_rsrc(a.k[1] + segB, 3 * 24576 + 3072), pVB = mk_rsrc(a.v[1] + segB, 3 * 24576 + 3072);
  const rsrc_t pWA = mk_rsrc(a.wq[0] + wqo, 79 * 12288 + 384), pWB = mk_rsrc(a.wq[1] + wqo, 79 * 12288 + 384);
  const int brw = j & 1, cq = j >> 1;
  const int lc = lane < 60 ? lane : 59, rr3 = lc / 20, c20 = lc - rr3 * 20;
  const rsrc_t pWO = mk_rsrc((brw ? a.wo[1] : a.wo[0]) + (size_t)(h * DH + 24 * w) * 1280 + cq * 320, 23 * 1280 + 320);
  const u32 wo_off = rr3 * 1280 + c20 * 16;

  u32x4 buf[NB][3];
  u32x4 wo[8];
  auto issue = [&](auto IC) {
    constexpr int n = decltype(IC)::value, ty = item_type(n), ix = item_idx(n);
    if constexpr (ty == T_WA || ty == T_WB) {
#pragma unroll
      for (int t = 0; t < 3; ++t) buf[n % NB][t] = ldb<AUX_DF>(ty == T_WA ? pWA : pWB, w_off + t * 128, ix * 12288);
    } else if constexpr (ty == T_WO) {
#pragma unroll
      for (int t = 0; t < 3; ++t) if (ix * 3 + t < 8) wo[ix * 3 + t] = ldb<AUX_DF>(pWO, wo_off, (ix * 3 + t) * 3840);
    } else if constexpr (ty != T_NONE) {
#pragma unroll
      for (int t = 0; t < 3; ++t) buf[n % NB][t] = ldb<AUX_NT>(ty == T_KA ? pKA : ty == T_VA ? pVA : ty == T_KB ? pKB : pVB, kv_off + t * 128, ix * 24576);
    }
  };
  __builtin_amdgcn_sched_barrier(0);
  static_for<NB>([&](auto IC) { issue(IC); __builtin_amdgcn_sched_barrier(0); });

  u32 msk[4];
#pragma unroll
  for (int d = 0; d < 4; ++d) msk[d] = (c >> 1) == d ? ((c & 1) ? 0xffff0000u : 0x0000ffffu) : 0u;
  const bool vlane = (lane >> 5) == ((lane >> 3) & 1);
  const int vofs = (lane >> 5) * DH + c * 8 + ((lane >> 4) & 1) * 4;

  f32x4 acc[3], accb[3];
  u32 q[12];
  float s[16], sb[4];
  u32 pa[16], pb[4];
#pragma unroll
  for (int t = 0; t < 3; ++t) { acc[t] = f32x4{0.f, 0.f, 0.f, 0.f}; accb[t] = f32x4{0.f, 0.f, 0.f, 0.f}; }

  auto axpy = [&](f32x4 (&ac)[3], u32 pp, const u32x4 (&tile)[3]) {
    const u32x4 af = {pp & msk[0], pp & msk[1], pp & msk[2], pp & msk[3]};
#pragma unroll
    for (int t = 0; t < 3; ++t)
      ac[t] = __builtin_amdgcn_mfma_f32_16x16x32_bf16(__builtin_bit_cast(bf16x8, af), __builtin_bit_cast(bf16x8, tile[t]), ac[t], 0, 0, 0);
  };
  auto put = [&](int base, f32x4 (&ac)[3]) {
    if (vlane) {
#pragma unroll
      for (int t = 0; t < 3; ++t) *(f32x4*)(lds + base + w * (2 * DH) + vofs + t * 64) = ac[t];
    }
  };
  auto qfin = [&](int br) {
    if (tid < 96) {
      float q0 = 0.f, q1 = 0.f;
#pragma unroll
      for (int k = 0; k < 16; ++k) { const float2 v = *(const float2*)(lds + L_QP + (br * 16 + k) * DH + 2 * tid); q0 += v.x; q1 += v.y; }
      ((u32*)lds)[L_Q + br * 96 + tid] = pk_bf16(q0, q1);
    }
  };
  auto qget = [&](int br) {
#pragma unroll
    for (int t = 0; t < 3; ++t) {
      const u32x4 v = *(const u32x4*)((const u32*)lds + L_Q + br * 96 + t * 32 + c * 4);
#pragma unroll
      for (int k = 0; k < 4; ++k) q[t * 4 + k] = v[k];
    }
  };
  auto score = [&](const u32x4 (&tile)[3]) {
    float d[3];
#pragma unroll
    for (int t = 0; t < 3; ++t) {
      d[t] = 0.f;
#pragma unroll
      for (int k = 0; k < 4; ++k) d[t] = dot2(tile[t][k], q[t * 4 + k], d[t]);
    }
    float x = (d[0] + d[1]) + d[2];
    x += dpp<XOR1>(x); x += dpp<XOR2>(x); x += dpp<HMIR>(x);
    return x;
  };

  static_for<NITEM>([&](auto IC) {
    constexpr int n = decltype(IC)::value, ty = item_type(n), ix = item_idx(n);
    if constexpr (n == 10) {
      put(L_QP, acc);
      if (tid == 0) { ((u32*)lds)[L_E] = (u32)epoch; ((u32*)lds)[L_E + 1] = (u32)(epoch >> 32); }
      __syncthreads();
      qfin(0);
      __syncthreads();
      qget(0);
#pragma unroll
      for (int t = 0; t < 3; ++t) acc[t] = f32x4{0.f, 0.f, 0.f, 0.f};
    }
    if constexpr (n == 30) put(L_QP + 16 * DH, accb);
    if constexpr (n == 36) {
      float m = s[0];
#pragma unroll
      for (int i = 1; i < 16; ++i) m = fmaxf(m, s[i]);
      m = xoct_max(m) * C2;
      float l = 0.f;
#pragma unroll
      for (int i = 0; i < 16; ++i) { const float p = __builtin_amdgcn_exp2f(fmaf(s[i], C2, -m)); pa[i] = pk_bf16(p, p); l += __uint_as_float(pa[i] & 0xffff0000u); }
      l = xoct_sum(l);
      if (lane == 0) { lds[L_ML + w] = m; lds[L_ML + 8 + w] = l; }
      __syncthreads();
      qfin(1);
#pragma unroll
      for (int t = 0; t < 3; ++t) accb[t] = f32x4{0.f, 0.f, 0.f, 0.f};
    }
    if constexpr (n == 52) {
      put(L_OP, acc);
      __syncthreads();
      qget(1);
    }
    if constexpr (n == 56) {
      float m = fmaxf(fmaxf(sb[0], sb[1]), fmaxf(sb[2], sb[3]));
      m = xoct_max(m) * C2;
      float l = 0.f;
#pragma unroll
      for (int i = 0; i < 4; ++i) { const float p = __builtin_amdgcn_exp2f(fmaf(sb[i], C2, -m)); pb[i] = pk_bf16(p, p); l += __uint_as_float(pb[i] & 0xffff0000u); }
      l = xoct_sum(l);
      if (lane == 0) { lds[L_ML + 16 + w] = m; lds[L_ML + 24 + w] = l; }
    }
    if constexpr (ty == T_WA || ty == T_WB) {
      const u32 xd = ty == T_WA ? xa[ix >> 1] : xb[ix >> 1];
      const u32 pp = __builtin_amdgcn_perm(xd, xd, (ix & 1) ? 0x03020302u : 0x01000100u);
      axpy(ty == T_WA ? acc : accb, pp, buf[n % NB]);
    } else if constexpr (ty == T_KA) s[ix] = score(buf[n % NB]);
    else if constexpr (ty == T_KB) sb[ix] = score(buf[n % NB]);
    else if constexpr (ty == T_VA) axpy(acc, pa[ix], buf[n % NB]);
    else if constexpr (ty == T_VB) axpy(accb, pb[ix], buf[n % NB]);
    __builtin_amdgcn_sched_barrier(0);
    issue(std::integral_constant<int, n + NB>{});
    __builtin_amdgcn_sched_barrier(0);
  });

  put(L_OP + 16 * DH, accb);
  __syncthreads();
  const u64 tag = ((((u64)((const u32*)lds)[L_E + 1] << 32) | ((const u32*)lds)[L_E]) + 1);
  const u32 tag_hi = (u32)(tag >> 8), tag_lo = (u32)tag << 24;
  gu64* g1 = (gu64*)(a.ws + WS_G1) + (size_t)h * (2 * 8 * G1S);
  if (tid < 2 * DH) {
    const int br = tid >= DH, d = tid - br * DH;
    const f32x4 m0 = *(const f32x4*)(lds + L_ML + br * 16), m1 = *(const f32x4*)(lds + L_ML + br * 16 + 4);
    const f32x4 l0 = *(const f32x4*)(lds + L_ML + br * 16 + 8), l1 = *(const f32x4*)(lds + L_ML + br * 16 + 12);
    const float M = fmaxf(fmaxf(fmaxf(m0[0], m0[1]), fmaxf(m0[2], m0[3])), fmaxf(fmaxf(m1[0], m1[1]), fmaxf(m1[2], m1[3])));
    float L = 0.f, o = 0.f;
#pragma unroll
    for (int k = 0; k < 8; ++k) {
      const float f = __builtin_amdgcn_exp2f((k < 4 ? m0[k & 3] : m1[k & 3]) - M);
      L = fmaf(f, k < 4 ? l0[k & 3] : l1[k & 3], L);
      const float* op = lds + L_OP + (br * 16 + 2 * k) * DH + d;
      o = fmaf(f, op[0] + op[DH], o);
    }
    gu64* dst = g1 + (br * 8 + j) * G1S;
    __hip_atomic_store(dst + d, gran(tag_hi, tag_lo, o / L), RLX_AGENT);
    if (d == 0) __hip_atomic_store(dst + DH, gran(tag_hi, tag_lo, M + __builtin_amdgcn_logf(L)), RLX_AGENT);
  }

  u32 ob;
  {
    const int lg = lane < 48 ? lane : 47, bb = lg >= 24, rr = lg - bb * 24;
    gu64* src = g1 + (brw * 8 + bb * 4) * G1S;
    u64 go[4], gl[4];
    for (unsigned spin = 0;; ++spin) {
#pragma unroll
      for (int k = 0; k < 4; ++k) { go[k] = __hip_atomic_load(src + k * G1S + 24 * w + rr, RLX_AGENT); gl[k] = __hip_atomic_load(src + k * G1S + DH, RLX_AGENT); }
      bool ok = true;
#pragma unroll
      for (int k = 0; k < 4; ++k) ok = ok && (go[k] >> 24) == (tag & 0xffffffffffull) && (gl[k] >> 24) == (tag & 0xffffffffffull);
      if (__all(ok) || spin > SPIN_CAP) break;
      __builtin_amdgcn_s_sleep(1);
    }
    float ls[4];
#pragma unroll
    for (int k = 0; k < 4; ++k) ls[k] = __uint_as_float((u32)gl[k] << 8);
    const float M = fmaxf(fmaxf(ls[0], ls[1]), fmaxf(ls[2], ls[3]));
    float L = 0.f, o = 0.f;
#pragma unroll
    for (int k = 0; k < 4; ++k) { const float f = __builtin_amdgcn_exp2f(ls[k] - M); L += f; o = fmaf(f, __uint_as_float((u32)go[k] << 8), o); }
    ob = pk_bf16(o / L, 0.f);
  }
  {
    float y0[8], y1[8];
#pragma unroll
    for (int k = 0; k < 8; ++k) { y0[k] = 0.f; y1[k] = 0.f; }
#pragma unroll
    for (int i = 0; i < 8; ++i) {
      const u32 o0 = (u32)__builtin_amdgcn_ds_bpermute((3 * i + rr3) * 4, (int)ob);
      const u32 o1 = (u32)__builtin_amdgcn_ds_bpermute((24 + 3 * i + rr3) * 4, (int)ob);
      const u32 o0h = o0 << 16, o1h = o1 << 16;
#pragma unroll
      for (int k = 0; k < 4; ++k) {
        y0[2 * k] = dot2(wo[i][k], o0, y0[2 * k]); y0[2 * k + 1] = dot2(wo[i][k], o0h, y0[2 * k + 1]);
        y1[2 * k] = dot2(wo[i][k], o1, y1[2 * k]); y1[2 * k + 1] = dot2(wo[i][k], o1h, y1[2 * k + 1]);
      }
    }
    if (lane < 60) {
      float* yp = lds + L_Y + (w * 3 + rr3) * 320 + c20 * 8;
      *(f32x4*)(yp) = f32x4{y0[0], y0[1], y0[2], y0[3]}; *(f32x4*)(yp + 4) = f32x4{y0[4], y0[5], y0[6], y0[7]};
      *(f32x4*)(yp + 160) = f32x4{y1[0], y1[1], y1[2], y1[3]}; *(f32x4*)(yp + 164) = f32x4{y1[4], y1[5], y1[6], y1[7]};
    }
  }
  __syncthreads();
  gu64* g2 = (gu64*)(a.ws + WS_G2);
  if (tid < 320) {
    float y = 0.f;
#pragma unroll
    for (int k = 0; k < 24; ++k) y += lds[L_Y + k * 320 + tid];
    const int bb = tid >= 160, col = tid - bb * 160;
    __hip_atomic_store(g2 + ((size_t)(brw * NH + h) * 2 + bb) * DM + cq * 160 + col, gran(tag_hi, tag_lo, y), RLX_AGENT);
  }
  if (w == 7 && wg < 160) {
    const int fbr = wg / 80, rem = wg - fbr * 80, fb = rem / 40, cb = rem - fb * 40;
    gu64* src = g2 + ((size_t)(fbr * NH + r) * 2 + fb) * DM + cb * 16 + 2 * c;
    u64 g[8];
    for (unsigned spin = 0;; ++spin) {
#pragma unroll
      for (int k = 0; k < 4; ++k) { g[2 * k] = __hip_atomic_load(src + (size_t)k * (8 * 2 * DM), RLX_AGENT); g[2 * k + 1] = __hip_atomic_load(src + (size_t)k * (8 * 2 * DM) + 1, RLX_AGENT); }
      bool ok = true;
#pragma unroll
      for (int k = 0; k < 8; ++k) ok = ok && (g[k] >> 24) == (tag & 0xffffffffffull);
      if (__all(ok) || spin > SPIN_CAP) break;
      __builtin_amdgcn_s_sleep(1);
    }
    float y0 = 0.f, y1 = 0.f;
#pragma unroll
    for (int k = 0; k < 4; ++k) { y0 += __uint_as_float((u32)g[2 * k] << 8); y1 += __uint_as_float((u32)g[2 * k + 1] << 8); }
    y0 = xoct_sum(y0); y1 = xoct_sum(y1);
    if (lane < 8) *(GAS u32*)(a.out + (fb * (2 * DM) + fbr * DM + cb * 16 + 2 * c) * 2) = pk_bf16(y0, y1);
    if (wg == 0 && lane == 0) __hip_atomic_store((gu64*)a.ws, tag, RLX_AGENT);
  }
}

extern "C" void kernel_launch(void* const* d_in, const int* in_sizes, int n_in, void* d_out, int out_size, void* d_ws,
                              size_t ws_size, hipStream_t stream) {
  Args a;
  for (int i = 0; i < 2; ++i) {
    a.x[i] = (const char*)d_in[5 * i + 0]; a.k[i] = (const char*)d_in[5 * i + 1]; a.v[i] = (const char*)d_in[5 * i + 2];
    a.wq[i] = (const char*)d_in[5 * i + 3]; a.wo[i] = (const char*)d_in[5 * i + 4];
  }
  a.out = (char*)d_out; a.ws = (char*)d_ws;
  hipLaunchKernelGGL(fused_decode, dim3(NWG), dim3(NTHR), 0, stream, a);
}
```

```cpp
#include <hip/hip_runtime.h>
#include <hip/hip_bf16.h>
#include <stdint.h>

#define GAS __attribute__((address_space(1)))
#define LAS __attribute__((address_space(3)))
typedef unsigned u32;
typedef unsigned long long u64;
typedef u32 u32x4 __attribute__((ext_vector_type(4)));
typedef float f32x4 __attribute__((ext_vector_type(4)));
typedef __bf16 bf16x2 __attribute__((ext_vector_type(2)));
typedef __bf16 bf16x8 __attribute__((ext_vector_type(8)));
typedef GAS u64 gu64;
typedef __amdgpu_buffer_rsrc_t rsrc_t;
#define RLX_AGENT __ATOMIC_RELAXED, __HIP_MEMORY_SCOPE_AGENT
#define SB() __builtin_amdgcn_sched_barrier(0)
#define FRESH() asm volatile("" ::: "memory")

#define TAU_A 22685
#define TAU_B 21475
#define LEAD 32
constexpr int QS = 16;
constexpr int LIM = 3;
constexpr int PT1 = 150;
constexpr int AUX_NT = 2, AUX_DEV = 16, AUX_DEV_NT = 18, AUX_SYS = 17;

constexpr int DM = 640, NH = 32, SK = 4096, DH = 192, WIN = 1024;
constexpr int NWG = 256, NTHR = 512;
constexpr float SCALE = 0.07216878364870322f, LOG2E = 1.4426950408889634f;
constexpr unsigned SPIN_CAP = 1u << 17;
constexpr u32 OOB = 0x7fffff00u;

constexpr int G1S = 208;
constexpr u32 WS_GQ = 4096, WS_G1 = WS_GQ + NH * 16 * DH * 8, WS_G2 = WS_G1 + NH * 16 * G1S * 8, WS_END = WS_G2 + 2 * NH * 2 * DM * 8;

struct Args {
  const char* x[2]; const char* k[2]; const char* v[2]; const char* wq[2]; const char* wo[2];
  char* out; char* ws;
  u32 tau_a, tau_b;
};

__device__ __forceinline__ float dot2(u32 a, u32 b, float c) {
  return __builtin_amdgcn_fdot2_f32_bf16(__builtin_bit_cast(bf16x2, a), __builtin_bit_cast(bf16x2, b), c, false);
}
template <int CTRL> __device__ __forceinline__ float dpp(float x) {
  return __builtin_bit_cast(float, __builtin_amdgcn_mov_dpp(__builtin_bit_cast(int, x), CTRL, 0xf, 0xf, true));
}
constexpr int XOR1 = 0xB1, XOR2 = 0x4E, HMIR = 0x141, ROR8 = 0x128;
__device__ __forceinline__ float x16_max(float x) {
  auto s = __builtin_amdgcn_permlane16_swap(__float_as_uint(x), __float_as_uint(x), false, false);
  return fmaxf(__uint_as_float(s[0]), __uint_as_float(s[1]));
}
__device__ __forceinline__ float x16_sum(float x) {
  auto s = __builtin_amdgcn_permlane16_swap(__float_as_uint(x), __float_as_uint(x), false, false);
  return __uint_as_float(s[0]) + __uint_as_float(s[1]);
}
__device__ __forceinline__ float xoct_max(float x) {
  x = x16_max(fmaxf(x, dpp<ROR8>(x)));
  auto t = __builtin_amdgcn_permlane32_swap(__float_as_uint(x), __float_as_uint(x), false, false);
  return fmaxf(__uint_as_float(t[0]), __uint_as_float(t[1]));
}
__device__ __forceinline__ float xoct_sum(float x) {
  x = x16_sum(x + dpp<ROR8>(x));
  auto t = __builtin_amdgcn_permlane32_swap(__float_as_uint(x), __float_as_uint(x), false, false);
  return __uint_as_float(t[0]) + __uint_as_float(t[1]);
}
__device__ __forceinline__ u32 pk_bf16(float lo, float hi) {
  bf16x2 r; r[0] = (__bf16)lo; r[1] = (__bf16)hi; return __builtin_bit_cast(u32, r);
}
__device__ __forceinline__ float rnd_bf16(float x) { return __uint_as_float(pk_bf16(0.f, x) & 0xffff0000u); }
__device__ __forceinline__ rsrc_t mk_rsrc(const char* p, u32 bytes) { return __builtin_amdgcn_make_buffer_rsrc((void*)p, 0, bytes, 0x00020000); }
__device__ __forceinline__ u32x4 ldnt(rsrc_t rs, u32 voff, u32 soff) { return __builtin_amdgcn_raw_buffer_load_b128(rs, voff, soff, AUX_NT); }
template <int N> __device__ __forceinline__ void wait_vm() { __builtin_amdgcn_s_waitcnt((N & 15) | (7 << 4) | (15 << 8) | ((N >> 4) << 14)); }
__device__ __forceinline__ u32 now() { return (u32)__builtin_amdgcn_s_memrealtime(); }

struct Tag { u32 lo, hi; };
template <int AUX> __device__ __forceinline__ void gstore2(rsrc_t rs, u32 off, Tag t, float v0, float v1) {
  const u32x4 g = {t.lo | ((__float_as_uint(v0) + 0x80u) >> 8), t.hi, t.lo | ((__float_as_uint(v1) + 0x80u) >> 8), t.hi};
  __builtin_amdgcn_raw_buffer_store_b128(g, rs, off, 0, AUX);
}
template <int AUX> __device__ __forceinline__ u32x4 gload2(rsrc_t rs, u32 off) { return __builtin_amdgcn_raw_buffer_load_b128(rs, off, 0, AUX); }
__device__ __forceinline__ u32 gbad(u32x4 g, Tag t) { return (g[1] ^ t.hi) | (g[3] ^ t.hi) | (((g[0] ^ t.lo) | (g[2] ^ t.lo)) >> 24); }
__device__ __forceinline__ float gv0(u32x4 g) { return __uint_as_float(g[0] << 8); }
__device__ __forceinline__ float gv1(u32x4 g) { return __uint_as_float(g[2] << 8); }
template <int AUX, int N> __device__ __forceinline__ void gpoll(rsrc_t rs, const u32 (&off)[N], u32 last_on, Tag t, u32x4 (&g)[N]) {
  u32 bad[N], any = 0;
#pragma unroll
  for (int k = 0; k < N; ++k) { bad[k] = gbad(g[k], t) & (k == N - 1 ? last_on : ~0u); any |= bad[k]; }
  if (__any(any)) {
    for (unsigned spin = 0; spin < SPIN_CAP; ++spin) {
      FRESH();
      u32x4 n[N];
#pragma unroll
      for (int k = 0; k < N; ++k) n[k] = gload2<AUX>(rs, bad[k] ? off[k] : OOB);
      SB();
      any = 0;
#pragma unroll
      for (int k = 0; k < N; ++k) { g[k] = bad[k] ? n[k] : g[k]; bad[k] = gbad(g[k], t) & (k == N - 1 ? last_on : ~0u); any |= bad[k]; }
      if (!__any(any)) break;
    }
  }
}

constexpr int L_QP = 0;
constexpr int L_ML = L_QP + 32 * DH;
constexpr int L_Q = L_ML + 32;
constexpr int L_E = L_Q + 2 * 96;
constexpr int L_Y = L_E + 4;
constexpr int L_ALL = 21 * 1024;
static_assert(L_Y + 8 * 320 <= L_ALL, "LDS map");

__global__ void __launch_bounds__(NTHR, 2) fused_decode(Args a) {
  __shared__ __attribute__((aligned(16))) float lds[L_ALL];
  const u32 t_start = now();
  const int tid = threadIdx.x, lane = tid & 63;
  const int w = __builtin_amdgcn_readfirstlane(tid >> 6);
  const int wg = blockIdx.x;
  const int h = (wg & 7) * 4 + (wg >> 6), j = (wg >> 3) & 7, b = j >> 2, qtr = j & 3;
  const int r = lane >> 3, c = lane & 7;
  const int brw = j & 1, cq = j >> 1;

  u32 xv[2][3];
  u32x4 W[3][3], KB[4][3], VB[4][3], KA[4][3], VA[4][3];
  const int row0 = 64 * cq + 8 * w, row1 = row0 + 256, row2 = 512 + 32 * cq + 4 * w;
  const rsrc_t rW = mk_rsrc((brw ? a.wq[1] : a.wq[0]) + h * 384, 639 * 12288 + 384);
  const u32 w_off = r * 12288 + c * 16, w_off2 = r < 4 ? w_off : OOB;
#pragma unroll
  for (int t = 0; t < 3; ++t) W[0][t] = ldnt(rW, w_off + t * 128, row0 * 12288);
  SB();
  {
    const rsrc_t rX = mk_rsrc(brw ? a.x[1] : a.x[0], 2 * DM * 2);
#pragma unroll
    for (int bb = 0; bb < 2; ++bb) {
      xv[bb][0] = __builtin_amdgcn_raw_buffer_load_b16(rX, r * 2, bb * 1280 + row0 * 2, AUX_NT);
      xv[bb][1] = __builtin_amdgcn_raw_buffer_load_b16(rX, r * 2, bb * 1280 + row1 * 2, AUX_NT);
      xv[bb][2] = __builtin_amdgcn_raw_buffer_load_b16(rX, r < 4 ? r * 2 : OOB, bb * 1280 + row2 * 2, AUX_NT);
    }
  }
  u64 epoch = 0;
  LAS u32* cnt = (LAS u32*)(lds + L_E + 2);
  if (tid == 0) { epoch = __hip_atomic_load((gu64*)a.ws, RLX_AGENT); cnt[0] = 0; cnt[1] = 0; }
  __syncthreads();
  auto arrive = [&](int k) {
    asm volatile("s_waitcnt lgkmcnt(0)" ::: "memory");
    if (lane == 0) __hip_atomic_fetch_add(cnt + k, 1u, __ATOMIC_RELAXED, __HIP_MEMORY_SCOPE_WORKGROUP);
  };
  auto await = [&](int k, u32 n) {
    while ((u32)__builtin_amdgcn_readfirstlane(__hip_atomic_load(cnt + k, __ATOMIC_RELAXED, __HIP_MEMORY_SCOPE_WORKGROUP)) < n) __builtin_amdgcn_s_sleep(1);
    FRESH();
  };

  constexpr u32 STR = 32 * 3072;
  const u32 kv_off = r * 384 + c * 16;
  const size_t segA = (size_t)(b * NH + h) * SK * 384 + (qtr * 8 + w) * 3072, segB = segA + (size_t)(SK - WIN) * 384;
  const rsrc_t rKA = mk_rsrc(a.k[0] + segA, 15 * STR + 3072), rVA = mk_rsrc(a.v[0] + segA, 15 * STR + 3072);
  const rsrc_t rKB = mk_rsrc(a.k[1] + segB, 3 * STR + 3072), rVB = mk_rsrc(a.v[1] + segB, 3 * STR + 3072);
  const u32 gid = (u32)(b * NH + h), rot = gid * 6 + (gid >> 4) * 2;
  auto ob = [&](u32 i) { return ((rot + i) & 3) * STR; };
  auto oa = [&](u32 i) { return ((rot + i) & 15) * STR; };
  u32 tau = a.tau_a;
  const u32 stagger = (tau * (u32)w) >> 3;
  u32 sched = ((tau * LEAD) >> 8) + stagger;
  auto slot = [&]() {
    while (now() - t_start < (sched >> 8)) __builtin_amdgcn_s_sleep(1);
    sched += tau;
  };
#define TILE(dst, rs, soff, L) do { SB(); slot(); wait_vm<L>(); \
    _Pragma("unroll") for (int t_ = 0; t_ < 3; ++t_) dst[t_] = ldnt(rs, kv_off + t_ * 128, soff); SB(); } while (0)

  u32 msk[4];
#pragma unroll
  for (int d = 0; d < 4; ++d) msk[d] = (c >> 1) == d ? ((c & 1) ? 0xffff0000u : 0x0000ffffu) : 0u;
  const bool vlane = (lane >> 5) == ((lane >> 3) & 1);
  const int vofs = (lane >> 5) * DH + c * 8 + ((lane >> 4) & 1) * 4;
  auto frag = [&](u32 pp) { return __builtin_bit_cast(bf16x8, u32x4{pp & msk[0], pp & msk[1], pp & msk[2], pp & msk[3]}); };
  auto mma = [&](f32x4& ac, bf16x8 af, u32x4 v) {
    ac = __builtin_amdgcn_mfma_f32_16x16x32_bf16(af, __builtin_bit_cast(bf16x8, v), ac, 0, 0, 0);
  };
  auto put = [&](int base, f32x4 (&ac)[3]) {
    if (vlane) {
#pragma unroll
      for (int t = 0; t < 3; ++t) *(f32x4*)(lds + base + vofs + t * 64) = ac[t];
    }
  };
#pragma unroll
  for (int i = 1; i < 3; ++i) {
    SB(); slot();
#pragma unroll
    for (int t = 0; t < 3; ++t) W[i][t] = ldnt(rW, (i == 1 ? w_off : w_off2) + t * 128, (i == 1 ? row1 : row2) * 12288);
    SB();
  }
  TILE(KB[0], rKB, ob(0), 63);
  f32x4 acc[3];
  {
    f32x4 ac1[3];
#pragma unroll
    for (int t = 0; t < 3; ++t) { acc[t] = f32x4{0.f, 0.f, 0.f, 0.f}; ac1[t] = f32x4{0.f, 0.f, 0.f, 0.f}; }
#pragma unroll
    for (int i = 0; i < 3; ++i) {
      const bf16x8 f0 = frag(xv[0][i] | (xv[0][i] << 16)), f1 = frag(xv[1][i] | (xv[1][i] << 16));
#pragma unroll
      for (int t = 0; t < 3; ++t) { mma(acc[t], f0, W[i][t]); mma(ac1[t], f1, W[i][t]); }
    }
    put(L_QP + (w * 4 + 0) * DH, acc);
    put(L_QP + (w * 4 + 2) * DH, ac1);
  }
  if (tid == 0) { ((u32*)lds)[L_E] = (u32)epoch; ((u32*)lds)[L_E + 1] = (u32)(epoch >> 32); }
  arrive(0);
  TILE(KB[1], rKB, ob(1), LIM);
  TILE(KB[2], rKB, ob(2), LIM);
  await(0, 8);
  const u64 tag = ((((u64)((const u32*)lds)[L_E + 1] << 32) | ((const u32*)lds)[L_E]) + 1);
  const Tag tg = {(u32)tag << 24, (u32)(tag >> 8)};
  const rsrc_t rG = mk_rsrc(a.ws, WS_END);
  const int tbr = tid >= 96, tp = tid - tbr * 96;
  if (w < 3) {
    float2 u[16];
#pragma unroll
    for (int k = 0; k < 16; ++k) u[k] = *(const float2*)(lds + L_QP + ((k >> 1) * 4 + tbr * 2 + (k & 1)) * DH + 2 * tp);
    SB();
    float q0 = 0.f, q1 = 0.f;
#pragma unroll
    for (int k = 0; k < 16; ++k) { q0 += u[k].x; q1 += u[k].y; }
    gstore2<AUX_DEV_NT>(rG, WS_GQ + (((h * 2 + brw) * 4 + cq) * 2 + tbr) * (DH * 8) + tp * 16, tg, q0, q1);
  }
  TILE(KB[3], rKB, ob(3), LIM + 1);
  TILE(VB[0], rVB, ob(0), LIM);
  u32x4 gq[4];
  const u32 qs0 = WS_GQ + ((h * 2 + tbr) * 8 + b) * (DH * 8) + tp * 16;
  const u32 qsrc[4] = {qs0, qs0 + 2 * DH * 8, qs0 + 4 * DH * 8, qs0 + 6 * DH * 8};
  const u32 qstretch = (tau * QS) >> 8;
  TILE(VB[1], rVB, ob(1), LIM);
  if (w < 3) gq[0] = gload2<AUX_DEV_NT>(rG, qsrc[0]);
  sched += qstretch;
  TILE(VB[2], rVB, ob(2), LIM + 1);
  if (w < 3) gq[1] = gload2<AUX_DEV_NT>(rG, qsrc[1]);
  sched += qstretch;
  TILE(VB[3], rVB, ob(3), LIM + 1);
  if (w < 3) gq[2] = gload2<AUX_DEV_NT>(rG, qsrc[2]);
  sched += qstretch;
  TILE(KA[0], rKA, oa(0), LIM + 1);
  if (w < 3) gq[3] = gload2<AUX_DEV_NT>(rG, qsrc[3]);
  sched += qstretch;
  TILE(KA[1], rKA, oa(1), LIM + 1);
  TILE(KA[2], rKA, oa(2), LIM);
  if (w < 3) {
    gpoll<AUX_DEV_NT, 4>(rG, qsrc, ~0u, tg, gq);
    ((u32*)lds)[L_Q + tid] = pk_bf16((gv0(gq[0]) + gv0(gq[1])) + (gv0(gq[2]) + gv0(gq[3])), (gv1(gq[0]) + gv1(gq[1])) + (gv1(gq[2]) + gv1(gq[3])));
    arrive(1);
  }
  TILE(KA[3], rKA, oa(3), LIM);
  await(1, 3);

  u32x4 q[3];
  auto qget = [&](int br) {
#pragma unroll
    for (int t = 0; t < 3; ++t) q[t] = *(const u32x4*)((const u32*)lds + L_Q + br * 96 + t * 32 + c * 4);
  };
  auto score = [&](const u32x4 (&k)[3]) {
    float d[3];
#pragma unroll
    for (int t = 0; t < 3; ++t) d[t] = dot2(k[t][3], q[t][3], dot2(k[t][2], q[t][2], dot2(k[t][1], q[t][1], dot2(k[t][0], q[t][0], 0.f))));
    float x = (d[0] + d[1]) + d[2];
    x += dpp<XOR1>(x); x += dpp<XOR2>(x); x += dpp<HMIR>(x);
    return rnd_bf16(rnd_bf16(x) * SCALE) * LOG2E;
  };
  float m, l;
  bf16x8 fb[4];
  auto block_sm = [&](const u32x4 (&kk)[4][3]) {
    float sb[4];
#pragma unroll
    for (int i = 0; i < 4; ++i) sb[i] = score(kk[i]);
    m = xoct_max(fmaxf(fmaxf(sb[0], sb[1]), fmaxf(sb[2], sb[3])));
    l = 0.f;
#pragma unroll
    for (int i = 0; i < 4; ++i) {
      const float p = __builtin_amdgcn_exp2f(sb[i] - m);
      const u32 pp = pk_bf16(p, p);
      l += __uint_as_float(pp & 0xffff0000u);
      fb[i] = frag(pp);
    }
  };
  u32x4 K[3], V[3];
  qget(1);
  block_sm(KB);
  qget(0);
  TILE(VA[0], rVA, oa(0), LIM);
#pragma unroll
  for (int t = 0; t < 3; ++t) {
    acc[t] = f32x4{0.f, 0.f, 0.f, 0.f};
#pragma unroll
    for (int i = 0; i < 4; ++i) mma(acc[t], fb[i], VB[i][t]);
  }
  l = xoct_sum(l);
  if (lane == 0) { lds[L_ML + 16 + w] = m; lds[L_ML + 24 + w] = l; }
  put(L_QP + (16 + w * 2) * DH, acc);
  TILE(VA[1], rVA, oa(1), LIM);
  block_sm(KA);
  TILE(VA[2], rVA, oa(2), LIM);
  TILE(VA[3], rVA, oa(3), LIM);
#pragma unroll
  for (int t = 0; t < 3; ++t) { acc[t] = f32x4{0.f, 0.f, 0.f, 0.f}; mma(acc[t], fb[0], VA[0][t]); mma(acc[t], fb[1], VA[1][t]); mma(acc[t], fb[2], VA[2][t]); }
  TILE(K, rKA, oa(4), LIM);
#pragma unroll
  for (int t = 0; t < 3; ++t) mma(acc[t], fb[3], VA[3][t]);
  tau = a.tau_b;
  bf16x8 fa;
  auto half1 = [&](u32 soff) {
    TILE(V, rVA, soff, LIM);
    const float s = score(K);
    const float mn = fmaxf(m, xoct_max(s));
    const float al = __uint_as_float(__builtin_amdgcn_readfirstlane(__float_as_uint(__builtin_amdgcn_exp2f(m - mn))));
    m = mn;
#pragma unroll
    for (int t = 0; t < 3; ++t) acc[t] *= al;
    const float p = __builtin_amdgcn_exp2f(s - m);
    const u32 pp = pk_bf16(p, p);
    l = fmaf(l, al, __uint_as_float(pp & 0xffff0000u));
    fa = frag(pp);
  };
#pragma unroll 1
  for (u32 i = 4; i < 15; ++i) {
    half1(oa(i));
    TILE(K, rKA, oa(i + 1), LIM);
#pragma unroll
    for (int t = 0; t < 3; ++t) mma(acc[t], fa, V[t]);
  }
  half1(oa(15));
  u32x4 wo[12];
  const rsrc_t rO = mk_rsrc((brw ? a.wo[1] : a.wo[0]) + (size_t)(h * DH + 8 * w) * 1280 + cq * 320, 135 * 1280 + 320);
  const u32 wo_off = (lane & 15) < 10 ? (lane >> 4) * 1280 + (lane & 15) * 16 : OOB;
  tau -= tau / 6;
  auto wo_ld = [&](int g) {
    SB(); slot();
#pragma unroll
    for (int i = 2 * g; i < 2 * g + 2; ++i) {
      wo[2 * i] = ldnt(rO, wo_off, g * (64 * 1280) + (i & 1) * 5120);
      wo[2 * i + 1] = ldnt(rO, wo_off + 160, g * (64 * 1280) + (i & 1) * 5120);
    }
    SB();
  };
  l = xoct_sum(l);
  if (lane == 0) { lds[L_ML + w] = m; lds[L_ML + 8 + w] = l; }
  sched -= stagger;
  wo_ld(0);
#pragma unroll
  for (int t = 0; t < 3; ++t) mma(acc[t], fa, V[t]);
  put(L_QP + (w * 2) * DH, acc);
  __syncthreads();

  const u32 t_pub = now();
  const u32 g1h = WS_G1 + h * (16 * G1S * 8);
  if (w < 3) {
    const f32x4 m0 = *(const f32x4*)(lds + L_ML + tbr * 16), m1 = *(const f32x4*)(lds + L_ML + tbr * 16 + 4);
    const f32x4 l0 = *(const f32x4*)(lds + L_ML + tbr * 16 + 8), l1 = *(const f32x4*)(lds + L_ML + tbr * 16 + 12);
    float2 u[16];
#pragma unroll
    for (int k = 0; k < 16; ++k) u[k] = *(const float2*)(lds + L_QP + (tbr * 16 + k) * DH + 2 * tp);
    SB();
    const float M = fmaxf(fmaxf(fmaxf(m0[0], m0[1]), fmaxf(m0[2], m0[3])), fmaxf(fmaxf(m1[0], m1[1]), fmaxf(m1[2], m1[3])));
    float L = 0.f, o0 = 0.f, o1 = 0.f;
#pragma unroll
    for (int k = 0; k < 8; ++k) {
      const float f = __builtin_amdgcn_exp2f((k < 4 ? m0[k & 3] : m1[k & 3]) - M);
      L = fmaf(f, k < 4 ? l0[k & 3] : l1[k & 3], L);
      o0 = fmaf(f, u[2 * k].x + u[2 * k + 1].x, o0); o1 = fmaf(f, u[2 * k].y + u[2 * k + 1].y, o1);
    }
    const float inv = __builtin_amdgcn_rcpf(L);
    const u32 dst = g1h + (tbr * 8 + j) * (G1S * 8);
    const int pos = 12 * ((tp >> 2) & 7) + 4 * (tp >> 5) + (tp & 3);
    gstore2<AUX_DEV>(rG, dst + pos * 16, tg, o0 * inv, o1 * inv);
    if (tp == 0) { const float lse = M + __builtin_amdgcn_logf(L); gstore2<AUX_DEV>(rG, dst + DH * 8, tg, lse, lse); }
  }
  wo_ld(1);
  wo_ld(2);
  u32 ov;
  {
    const u32 rec = g1h + (brw * 8 + r) * (G1S * 8);
    const u32 src[3] = {rec + DH * 8, rec + (24 * w + 2 * c) * 8, c < 4 ? rec + (24 * w + 16 + 2 * c) * 8 : OOB};
    while (now() - t_pub < ((a.tau_b * PT1) >> 16)) __builtin_amdgcn_s_sleep(1);
    u32x4 g[3];
#pragma unroll
    for (int k = 0; k < 3; ++k) g[k] = gload2<AUX_DEV>(rG, src[k]);
    gpoll<AUX_DEV, 3>(rG, src, c < 4 ? ~0u : 0u, tg, g);
    const float lse = gv0(g[0]);
    const float f = __builtin_amdgcn_exp2f(lse - x16_max(fmaxf(lse, dpp<ROR8>(lse))));
    float v[5] = {f, f * gv0(g[1]), f * gv1(g[1]), f * gv0(g[2]), f * gv1(g[2])};
#pragma unroll
    for (int k = 0; k < 5; ++k) v[k] += dpp<ROR8>(v[k]);
#pragma unroll
    for (int k = 0; k < 5; ++k) v[k] = x16_sum(v[k]);
    const float inv = __builtin_amdgcn_rcpf(v[0]);
    ov = (lane & 8) ? pk_bf16(v[3] * inv, v[4] * inv) : pk_bf16(v[1] * inv, v[2] * inv);
  }
  {
    f32x4 y[4] = {f32x4{0.f, 0.f, 0.f, 0.f}, f32x4{0.f, 0.f, 0.f, 0.f}, f32x4{0.f, 0.f, 0.f, 0.f}, f32x4{0.f, 0.f, 0.f, 0.f}};
    const int sel = (((lane >> 3) & 1) * 32 + (lane >> 5)) * 4;
    const u32 psel = (lane & 16) ? 0x03020302u : 0x01000100u;
    u32 pr[6];
#pragma unroll
    for (int i = 0; i < 6; ++i) pr[i] = (u32)__builtin_amdgcn_ds_bpermute(sel + 8 * i, (int)ov);
    SB();
#pragma unroll
    for (int i = 0; i < 6; ++i) {
      const bf16x8 af = frag(__builtin_amdgcn_perm(pr[i], pr[i], psel));
      mma(y[2 * (i & 1)], af, wo[2 * i]); mma(y[2 * (i & 1) + 1], af, wo[2 * i + 1]);
    }
    if ((lane & 15) < 10) {
      float* yp = lds + L_Y + w * 320 + (lane >> 5) * 160 + (lane & 15) * 8 + ((lane >> 4) & 1) * 4;
      *(f32x4*)yp = y[0] + y[2]; *(f32x4*)(yp + 80) = y[1] + y[3];
    }
  }
  __syncthreads();
  if (tid < 160) {
    float2 u[8];
#pragma unroll
    for (int k = 0; k < 8; ++k) u[k] = *(const float2*)(lds + L_Y + k * 320 + 2 * tid);
    SB();
    const float y0 = ((u[0].x + u[1].x) + (u[2].x + u[3].x)) + ((u[4].x + u[5].x) + (u[6].x + u[7].x));
    const float y1 = ((u[0].y + u[1].y) + (u[2].y + u[3].y)) + ((u[4].y + u[5].y) + (u[6].y + u[7].y));
    const int bb = tid >= 80, p = tid - bb * 80;
    gstore2<AUX_DEV>(rG, WS_G2 + (((brw * NH + h) * 2 + bb) * DM + cq * 160 + 2 * p) * 8, tg, y0, y1);
  }
  if (w == 7 && wg < 160) {
    const int fbr = wg / 80, rem = wg - fbr * 80, fb2 = rem / 40, cb = rem - fb2 * 40;
    const u32 s0 = WS_G2 + (((fbr * NH + r) * 2 + fb2) * DM + cb * 16 + 2 * c) * 8;
    const u32 src[4] = {s0, s0 + 8 * 2 * DM * 8, s0 + 16 * 2 * DM * 8, s0 + 24 * 2 * DM * 8};
    __builtin_amdgcn_s_sleep(8);
    u32x4 g[4];
#pragma unroll
    for (int k = 0; k < 4; ++k) g[k] = gload2<AUX_DEV>(rG, src[k]);
    gpoll<AUX_DEV, 4>(rG, src, ~0u, tg, g);
    const float y0 = xoct_sum((gv0(g[0]) + gv0(g[1])) + (gv0(g[2]) + gv0(g[3])));
    const float y1 = xoct_sum((gv1(g[0]) + gv1(g[1])) + (gv1(g[2]) + gv1(g[3])));
    __builtin_amdgcn_raw_buffer_store_b32(pk_bf16(y0, y1), mk_rsrc(a.out, 2 * 2 * DM * 2),
                                          lane < 8 ? (fb2 * (2 * DM) + fbr * DM + cb * 16 + 2 * c) * 2 : OOB, 0, AUX_SYS);
    if (wg == 0 && lane == 0) __hip_atomic_store((gu64*)a.ws, tag, RLX_AGENT);
  }
}

extern "C" void kernel_launch(void* const* d_in, const int* in_sizes, int n_in, void* d_out, int out_size, void* d_ws,
                              size_t ws_size, hipStream_t stream) {
  Args a;
  for (int i = 0; i < 2; ++i) {
    a.x[i] = (const char*)d_in[5 * i + 0]; a.k[i] = (const char*)d_in[5 * i + 1]; a.v[i] = (const char*)d_in[5 * i + 2];
    a.wq[i] = (const char*)d_in[5 * i + 3]; a.wo[i] = (const char*)d_in[5 * i + 4];
  }
  a.out = (char*)d_out; a.ws = (char*)d_ws;
  int dev = 0, khz = 0;
  if (hipGetDevice(&dev) != hipSuccess || hipDeviceGetAttribute(&khz, hipDeviceAttributeWallClockRate, dev) != hipSuccess ||
      khz < 10000 || khz > 400000) khz = 100000;
  a.tau_a = (u32)((u64)TAU_A * (u64)khz / 100000u);
  a.tau_b = (u32)((u64)TAU_B * (u64)khz / 100000u);
  hipLaunchKernelGGL(fused_decode, dim3(NWG), dim3(NTHR), 0, stream, a);
}
```

```cpp
#include <hip/hip_runtime.h>
#include <hip/hip_bf16.h>
#include <stdint.h>

#define GAS __attribute__((address_space(1)))
#define LAS __attribute__((address_space(3)))
typedef unsigned u32;
typedef unsigned long long u64;
typedef u32 u32x4 __attribute__((ext_vector_type(4)));
typedef float f32x4 __attribute__((ext_vector_type(4)));
typedef __bf16 bf16x2 __attribute__((ext_vector_type(2)));
typedef __bf16 bf16x8 __attribute__((ext_vector_type(8)));
typedef GAS u64 gu64;
typedef __amdgpu_buffer_rsrc_t rsrc_t;
#define RLX_AGENT __ATOMIC_RELAXED, __HIP_MEMORY_SCOPE_AGENT
#define SB() __builtin_amdgcn_sched_barrier(0)
#define FRESH() asm volatile("" ::: "memory")

#define TAU_A 22685
#define TAU_B 22063
#define LEAD 32
constexpr int QS = 16;
constexpr int LIM = 3;
constexpr int PT1 = 150;
constexpr int AUX_NT = 2, AUX_DEV = 16, AUX_DEV_NT = 18, AUX_SYS = 17;

constexpr int DM = 640, NH = 32, SK = 4096, DH = 192, WIN = 1024;
constexpr int NWG = 256, NTHR = 512;
constexpr float SCALE = 0.07216878364870322f, LOG2E = 1.4426950408889634f;
constexpr unsigned SPIN_CAP = 1u << 17;
constexpr u32 OOB = 0x7fffff00u;

constexpr int G1S = 208;
constexpr u32 WS_GQ = 4096, WS_G1 = WS_GQ + NH * 16 * DH * 8, WS_G2 = WS_G1 + NH * 16 * G1S * 8, WS_END = WS_G2 + 2 * NH * 2 * DM * 8;

struct Args {
  const char* x[2]; const char* k[2]; const char* v[2]; const char* wq[2]; const char* wo[2];
  char* out; char* ws;
  u32 tau_a, tau_b;
};

__device__ __forceinline__ float dot2(u32 a, u32 b, float c) {
  return __builtin_amdgcn_fdot2_f32_bf16(__builtin_bit_cast(bf16x2, a), __builtin_bit_cast(bf16x2, b), c, false);
}
template <int CTRL> __device__ __forceinline__ float dpp(float x) {
  return __builtin_bit_cast(float, __builtin_amdgcn_mov_dpp(__builtin_bit_cast(int, x), CTRL, 0xf, 0xf, true));
}
constexpr int XOR1 = 0xB1, XOR2 = 0x4E, HMIR = 0x141, ROR8 = 0x128;
__device__ __forceinline__ float x16_max(float x) {
  auto s = __builtin_amdgcn_permlane16_swap(__float_as_uint(x), __float_as_uint(x), false, false);
  return fmaxf(__uint_as_float(s[0]), __uint_as_float(s[1]));
}
__device__ __forceinline__ float x16_sum(float x) {
  auto s = __builtin_amdgcn_permlane16_swap(__float_as_uint(x), __float_as_uint(x), false, false);
  return __uint_as_float(s[0]) + __uint_as_float(s[1]);
}
__device__ __forceinline__ float xoct_max(float x) {
  x = x16_max(fmaxf(x, dpp<ROR8>(x)));
  auto t = __builtin_amdgcn_permlane32_swap(__float_as_uint(x), __float_as_uint(x), false, false);
  return fmaxf(__uint_as_float(t[0]), __uint_as_float(t[1]));
}
__device__ __forceinline__ float xoct_sum(float x) {
  x = x16_sum(x + dpp<ROR8>(x));
  auto t = __builtin_amdgcn_permlane32_swap(__float_as_uint(x), __float_as_uint(x), false, false);
  return __uint_as_float(t[0]) + __uint_as_float(t[1]);
}
__device__ __forceinline__ u32 pk_bf16(float lo, float hi) {
  bf16x2 r; r[0] = (__bf16)lo; r[1] = (__bf16)hi; return __builtin_bit_cast(u32, r);
}
__device__ __forceinline__ float rnd_bf16(float x) { return __uint_as_float(pk_bf16(0.f, x) & 0xffff0000u); }
__device__ __forceinline__ rsrc_t mk_rsrc(const char* p, u32 bytes) { return __builtin_amdgcn_make_buffer_rsrc((void*)p, 0, bytes, 0x00020000); }
__device__ __forceinline__ u32x4 ldnt(rsrc_t rs, u32 voff, u32 soff) { return __builtin_amdgcn_raw_buffer_load_b128(rs, voff, soff, AUX_NT); }
template <int N> __device__ __forceinline__ void wait_vm() { __builtin_amdgcn_s_waitcnt((N & 15) | (7 << 4) | (15 << 8) | ((N >> 4) << 14)); }
__device__ __forceinline__ u32 now() { return (u32)__builtin_amdgcn_s_memrealtime(); }

struct Tag { u32 lo, hi; };
template <int AUX> __device__ __forceinline__ void gstore2(rsrc_t rs, u32 off, Tag t, float v0, float v1) {
  const u32x4 g = {t.lo | ((__float_as_uint(v0) + 0x80u) >> 8), t.hi, t.lo | ((__float_as_uint(v1) + 0x80u) >> 8), t.hi};
  __builtin_amdgcn_raw_buffer_store_b128(g, rs, off, 0, AUX);
}
template <int AUX> __device__ __forceinline__ u32x4 gload2(rsrc_t rs, u32 off) { return __builtin_amdgcn_raw_buffer_load_b128(rs, off, 0, AUX); }
__device__ __forceinline__ u32 gbad(u32x4 g, Tag t) { return (g[1] ^ t.hi) | (g[3] ^ t.hi) | (((g[0] ^ t.lo) | (g[2] ^ t.lo)) >> 24); }
__device__ __forceinline__ float gv0(u32x4 g) { return __uint_as_float(g[0] << 8); }
__device__ __forceinline__ float gv1(u32x4 g) { return __uint_as_float(g[2] << 8); }
template <int AUX, int N> __device__ __forceinline__ void gpoll(rsrc_t rs, const u32 (&off)[N], u32 last_on, Tag t, u32x4 (&g)[N]) {
  u32 bad[N], any = 0;
#pragma unroll
  for (int k = 0; k < N; ++k) { bad[k] = gbad(g[k], t) & (k == N - 1 ? last_on : ~0u); any |= bad[k]; }
  if (__any(any)) {
    for (unsigned spin = 0; spin < SPIN_CAP; ++spin) {
      FRESH();
      u32x4 n[N];
#pragma unroll
      for (int k = 0; k < N; ++k) n[k] = gload2<AUX>(rs, bad[k] ? off[k] : OOB);
      SB();
      any = 0;
#pragma unroll
      for (int k = 0; k < N; ++k) { g[k] = bad[k] ? n[k] : g[k]; bad[k] = gbad(g[k], t) & (k == N - 1 ? last_on : ~0u); any |= bad[k]; }
      if (!__any(any)) break;
    }
  }
}

constexpr int L_QP = 0;
constexpr int L_ML = L_QP + 32 * DH;
constexpr int L_Q = L_ML + 32;
constexpr int L_E = L_Q + 2 * 96;
constexpr int L_Y = L_E + 4;
constexpr int L_ALL = 21 * 1024;
static_assert(L_Y + 8 * 320 <= L_ALL, "LDS map");

__global__ void __launch_bounds__(NTHR, 2) fused_decode(Args a) {
  __shared__ __attribute__((aligned(16))) float lds[L_ALL];
  const u32 t_start = now();
  const int tid = threadIdx.x, lane = tid & 63;
  const int w = __builtin_amdgcn_readfirstlane(tid >> 6);
  const int wg = blockIdx.x;
  const int h = (wg & 7) * 4 + (wg >> 6), j = (wg >> 3) & 7, b = j >> 2, qtr = j & 3;
  const int r = lane >> 3, c = lane & 7;
  const int brw = j & 1, cq = j >> 1;

  u32 xv[2][3];
  u32x4 W[3][3], KB[4][3], VB[4][3], KA[4][3], VA[4][3];
  const int row0 = 64 * cq + 8 * w, row1 = row0 + 256, row2 = 512 + 32 * cq + 4 * w;
  const rsrc_t rW = mk_rsrc((brw ? a.wq[1] : a.wq[0]) + h * 384, 639 * 12288 + 384);
  const u32 w_off = r * 12288 + c * 16, w_off2 = r < 4 ? w_off : OOB;
#pragma unroll
  for (int t = 0; t < 3; ++t) W[0][t] = ldnt(rW, w_off + t * 128, row0 * 12288);
  SB();
  {
    const rsrc_t rX = mk_rsrc(brw ? a.x[1] : a.x[0], 2 * DM * 2);
#pragma unroll
    for (int bb = 0; bb < 2; ++bb) {
      xv[bb][0] = __builtin_amdgcn_raw_buffer_load_b16(rX, r * 2, bb * 1280 + row0 * 2, AUX_NT);
      xv[bb][1] = __builtin_amdgcn_raw_buffer_load_b16(rX, r * 2, bb * 1280 + row1 * 2, AUX_NT);
      xv[bb][2] = __builtin_amdgcn_raw_buffer_load_b16(rX, r < 4 ? r * 2 : OOB, bb * 1280 + row2 * 2, AUX_NT);
    }
  }
  u64 epoch = 0;
  LAS u32* cnt = (LAS u32*)(lds + L_E + 2);
  if (tid == 0) { epoch = __hip_atomic_load((gu64*)a.ws, RLX_AGENT); cnt[0] = 0; cnt[1] = 0; }
  __syncthreads();
  auto arrive = [&](int k) {
    asm volatile("s_waitcnt lgkmcnt(0)" ::: "memory");
    if (lane == 0) __hip_atomic_fetch_add(cnt + k, 1u, __ATOMIC_RELAXED, __HIP_MEMORY_SCOPE_WORKGROUP);
  };
  auto await = [&](int k, u32 n) {
    while ((u32)__builtin_amdgcn_readfirstlane(__hip_atomic_load(cnt + k, __ATOMIC_RELAXED, __HIP_MEMORY_SCOPE_WORKGROUP)) < n) __builtin_amdgcn_s_sleep(1);
    FRESH();
  };

  constexpr u32 STR = 32 * 3072;
  const u32 kv_off = r * 384 + c * 16;
  const size_t segA = (size_t)(b * NH + h) * SK * 384 + (qtr * 8 + w) * 3072, segB = segA + (size_t)(SK - WIN) * 384;
  const rsrc_t rKA = mk_rsrc(a.k[0] + segA, 15 * STR + 3072), rVA = mk_rsrc(a.v[0] + segA, 15 * STR + 3072);
  const rsrc_t rKB = mk_rsrc(a.k[1] + segB, 3 * STR + 3072), rVB = mk_rsrc(a.v[1] + segB, 3 * STR + 3072);
  const u32 gid = (u32)(b * NH + h), rot = gid * 6 + (gid >> 4) * 2;
  auto ob = [&](u32 i) { return ((rot + i) & 3) * STR; };
  auto oa = [&](u32 i) { return ((rot + i) & 15) * STR; };
  u32 tau = a.tau_a;
  const u32 stagger = (tau * (u32)w) >> 3;
  u32 sched = ((tau * LEAD) >> 8) + stagger;
  auto slot = [&]() {
    while (now() - t_start < (sched >> 8)) __builtin_amdgcn_s_sleep(1);
    sched += tau;
  };
#define TILE(dst, rs, soff, L) do { SB(); slot(); wait_vm<L>(); \
    _Pragma("unroll") for (int t_ = 0; t_ < 3; ++t_) dst[t_] = ldnt(rs, kv_off + t_ * 128, soff); SB(); } while (0)

  u32 msk[4];
#pragma unroll
  for (int d = 0; d < 4; ++d) msk[d] = (c >> 1) == d ? ((c & 1) ? 0xffff0000u : 0x0000ffffu) : 0u;
  const bool vlane = (lane >> 5) == ((lane >> 3) & 1);
  const int vofs = (lane >> 5) * DH + c * 8 + ((lane >> 4) & 1) * 4;
  auto frag = [&](u32 pp) { return __builtin_bit_cast(bf16x8, u32x4{pp & msk[0], pp & msk[1], pp & msk[2], pp & msk[3]}); };
  auto mma = [&](f32x4& ac, bf16x8 af, u32x4 v) {
    ac = __builtin_amdgcn_mfma_f32_16x16x32_bf16(af, __builtin_bit_cast(bf16x8, v), ac, 0, 0, 0);
  };
  auto put = [&](int base, f32x4 (&ac)[3]) {
    if (vlane) {
#pragma unroll
      for (int t = 0; t < 3; ++t) *(f32x4*)(lds + base + vofs + t * 64) = ac[t];
    }
  };
#pragma unroll
  for (int i = 1; i < 3; ++i) {
    SB(); slot();
#pragma unroll
    for (int t = 0; t < 3; ++t) W[i][t] = ldnt(rW, (i == 1 ? w_off : w_off2) + t * 128, (i == 1 ? row1 : row2) * 12288);
    SB();
  }
  TILE(KB[0], rKB, ob(0), 63);
  f32x4 acc[3];
  {
    f32x4 ac1[3];
#pragma unroll
    for (int t = 0; t < 3; ++t) { acc[t] = f32x4{0.f, 0.f, 0.f, 0.f}; ac1[t] = f32x4{0.f, 0.f, 0.f, 0.f}; }
#pragma unroll
    for (int i = 0; i < 3; ++i) {
      const bf16x8 f0 = frag(xv[0][i] | (xv[0][i] << 16)), f1 = frag(xv[1][i] | (xv[1][i] << 16));
#pragma unroll
      for (int t = 0; t < 3; ++t) { mma(acc[t], f0, W[i][t]); mma(ac1[t], f1, W[i][t]); }
    }
    put(L_QP + (w * 4 + 0) * DH, acc);
    put(L_QP + (w * 4 + 2) * DH, ac1);
  }
  if (tid == 0) { ((u32*)lds)[L_E] = (u32)epoch; ((u32*)lds)[L_E + 1] = (u32)(epoch >> 32); }
  arrive(0);
  TILE(KA[0], rKA, oa(0), LIM);
  TILE(KB[1], rKB, ob(1), LIM);
  await(0, 8);
  const u64 tag = ((((u64)((const u32*)lds)[L_E + 1] << 32) | ((const u32*)lds)[L_E]) + 1);
  const Tag tg = {(u32)tag << 24, (u32)(tag >> 8)};
  const rsrc_t rG = mk_rsrc(a.ws, WS_END);
  const int tbr = tid >= 96, tp = tid - tbr * 96;
  if (w < 3) {
    float2 u[16];
#pragma unroll
    for (int k = 0; k < 16; ++k) u[k] = *(const float2*)(lds + L_QP + ((k >> 1) * 4 + tbr * 2 + (k & 1)) * DH + 2 * tp);
    SB();
    float q0 = 0.f, q1 = 0.f;
#pragma unroll
    for (int k = 0; k < 16; ++k) { q0 += u[k].x; q1 += u[k].y; }
    gstore2<AUX_DEV_NT>(rG, WS_GQ + (((h * 2 + brw) * 4 + cq) * 2 + tbr) * (DH * 8) + tp * 16, tg, q0, q1);
  }
  TILE(KA[1], rKA, oa(1), LIM + 1);
  TILE(KB[2], rKB, ob(2), LIM);
  u32x4 gq[4];
  const u32 qs0 = WS_GQ + ((h * 2 + tbr) * 8 + b) * (DH * 8) + tp * 16;
  const u32 qsrc[4] = {qs0, qs0 + 2 * DH * 8, qs0 + 4 * DH * 8, qs0 + 6 * DH * 8};
  const u32 qstretch = (tau * QS) >> 8;
  TILE(KA[2], rKA, oa(2), LIM);
  if (w < 3) gq[0] = gload2<AUX_DEV_NT>(rG, qsrc[0]);
  sched += qstretch;
  TILE(KB[3], rKB, ob(3), LIM + 1);
  if (w < 3) gq[1] = gload2<AUX_DEV_NT>(rG, qsrc[1]);
  sched += qstretch;
  TILE(KA[3], rKA, oa(3), LIM + 1);
  if (w < 3) gq[2] = gload2<AUX_DEV_NT>(rG, qsrc[2]);
  sched += qstretch;
  TILE(VB[0], rVB, ob(0), LIM + 1);
  if (w < 3) gq[3] = gload2<AUX_DEV_NT>(rG, qsrc[3]);
  sched += qstretch;
  TILE(VA[0], rVA, oa(0), LIM + 1);
  TILE(VB[1], rVB, ob(1), LIM);
  if (w < 3) {
    gpoll<AUX_DEV_NT, 4>(rG, qsrc, ~0u, tg, gq);
    ((u32*)lds)[L_Q + tid] = pk_bf16((gv0(gq[0]) + gv0(gq[1])) + (gv0(gq[2]) + gv0(gq[3])), (gv1(gq[0]) + gv1(gq[1])) + (gv1(gq[2]) + gv1(gq[3])));
    arrive(1);
  }
  TILE(VA[1], rVA, oa(1), LIM);
  await(1, 3);

  u32x4 q[3];
  auto qget = [&](int br) {
#pragma unroll
    for (int t = 0; t < 3; ++t) q[t] = *(const u32x4*)((const u32*)lds + L_Q + br * 96 + t * 32 + c * 4);
  };
  auto score = [&](const u32x4 (&k)[3]) {
    float d[3];
#pragma unroll
    for (int t = 0; t < 3; ++t) d[t] = dot2(k[t][3], q[t][3], dot2(k[t][2], q[t][2], dot2(k[t][1], q[t][1], dot2(k[t][0], q[t][0], 0.f))));
    float x = (d[0] + d[1]) + d[2];
    x += dpp<XOR1>(x); x += dpp<XOR2>(x); x += dpp<HMIR>(x);
    return rnd_bf16(rnd_bf16(x) * SCALE) * LOG2E;
  };
  float m, l;
  auto block_sm = [&](const u32x4 (&kk)[4][3], u32 (&pp)[4]) {
    float sb[4];
#pragma unroll
    for (int i = 0; i < 4; ++i) sb[i] = score(kk[i]);
    m = xoct_max(fmaxf(fmaxf(sb[0], sb[1]), fmaxf(sb[2], sb[3])));
    l = 0.f;
#pragma unroll
    for (int i = 0; i < 4; ++i) {
      const float p = __builtin_amdgcn_exp2f(sb[i] - m);
      pp[i] = pk_bf16(p, p);
      l += __uint_as_float(pp[i] & 0xffff0000u);
    }
  };
  auto axpy = [&](f32x4 (&ac)[3], u32 pp, const u32x4 (&v)[3]) {
    const bf16x8 f = frag(pp);
#pragma unroll
    for (int t = 0; t < 3; ++t) mma(ac[t], f, v[t]);
  };
  u32x4 K[3], V[3];
  u32 pb[4], pa[4];
  f32x4 accb[3];
  qget(1);
  block_sm(KB, pb);
  qget(0);
  TILE(VB[2], rVB, ob(2), LIM);
  l = xoct_sum(l);
  if (lane == 0) { lds[L_ML + 16 + w] = m; lds[L_ML + 24 + w] = l; }
  block_sm(KA, pa);
  TILE(VA[2], rVA, oa(2), LIM);
#pragma unroll
  for (int t = 0; t < 3; ++t) { acc[t] = f32x4{0.f, 0.f, 0.f, 0.f}; accb[t] = f32x4{0.f, 0.f, 0.f, 0.f}; }
  axpy(accb, pb[0], VB[0]); axpy(acc, pa[0], VA[0]); axpy(accb, pb[1], VB[1]); axpy(acc, pa[1], VA[1]); axpy(accb, pb[2], VB[2]);
  TILE(VB[3], rVB, ob(3), LIM);
  axpy(acc, pa[2], VA[2]);
  TILE(VA[3], rVA, oa(3), LIM);
  axpy(accb, pb[3], VB[3]);
  put(L_QP + (16 + w * 2) * DH, accb);
  TILE(K, rKA, oa(4), LIM);
  axpy(acc, pa[3], VA[3]);
  tau = a.tau_b;
  bf16x8 fa;
  auto half1 = [&](u32 soff) {
    TILE(V, rVA, soff, LIM);
    const float s = score(K);
    const float mn = fmaxf(m, xoct_max(s));
    const float al = __uint_as_float(__builtin_amdgcn_readfirstlane(__float_as_uint(__builtin_amdgcn_exp2f(m - mn))));
    m = mn;
#pragma unroll
    for (int t = 0; t < 3; ++t) acc[t] *= al;
    const float p = __builtin_amdgcn_exp2f(s - m);
    const u32 pp = pk_bf16(p, p);
    l = fmaf(l, al, __uint_as_float(pp & 0xffff0000u));
    fa = frag(pp);
  };
#pragma unroll 1
  for (u32 i = 4; i < 15; ++i) {
    half1(oa(i));
    TILE(K, rKA, oa(i + 1), LIM);
#pragma unroll
    for (int t = 0; t < 3; ++t) mma(acc[t], fa, V[t]);
  }
  half1(oa(15));
  u32x4 wo[12];
  const rsrc_t rO = mk_rsrc((brw ? a.wo[1] : a.wo[0]) + (size_t)(h * DH + 8 * w) * 1280 + cq * 320, 135 * 1280 + 320);
  const u32 wo_off = (lane & 15) < 10 ? (lane >> 4) * 1280 + (lane & 15) * 16 : OOB;
  tau -= tau / 6;
  auto wo_ld = [&](int g) {
    SB(); slot();
#pragma unroll
    for (int i = 2 * g; i < 2 * g + 2; ++i) {
      wo[2 * i] = ldnt(rO, wo_off, g * (64 * 1280) + (i & 1) * 5120);
      wo[2 * i + 1] = ldnt(rO, wo_off + 160, g * (64 * 1280) + (i & 1) * 5120);
    }
    SB();
  };
  l = xoct_sum(l);
  if (lane == 0) { lds[L_ML + w] = m; lds[L_ML + 8 + w] = l; }
  sched -= stagger;
  wo_ld(0);
#pragma unroll
  for (int t = 0; t < 3; ++t) mma(acc[t], fa, V[t]);
  put(L_QP + (w * 2) * DH, acc);
  __syncthreads();

  const u32 t_pub = now();
  const u32 g1h = WS_G1 + h * (16 * G1S * 8);
  if (w < 3) {
    const f32x4 m0 = *(const f32x4*)(lds + L_ML + tbr * 16), m1 = *(const f32x4*)(lds + L_ML + tbr * 16 + 4);
    const f32x4 l0 = *(const f32x4*)(lds + L_ML + tbr * 16 + 8), l1 = *(const f32x4*)(lds + L_ML + tbr * 16 + 12);
    float2 u[16];
#pragma unroll
    for (int k = 0; k < 16; ++k) u[k] = *(const float2*)(lds + L_QP + (tbr * 16 + k) * DH + 2 * tp);
    SB();
    const float M = fmaxf(fmaxf(fmaxf(m0[0], m0[1]), fmaxf(m0[2], m0[3])), fmaxf(fmaxf(m1[0], m1[1]), fmaxf(m1[2], m1[3])));
    float L = 0.f, o0 = 0.f, o1 = 0.f;
#pragma unroll
    for (int k = 0; k < 8; ++k) {
      const float f = __builtin_amdgcn_exp2f((k < 4 ? m0[k & 3] : m1[k & 3]) - M);
      L = fmaf(f, k < 4 ? l0[k & 3] : l1[k & 3], L);
      o0 = fmaf(f, u[2 * k].x + u[2 * k + 1].x, o0); o1 = fmaf(f, u[2 * k].y + u[2 * k + 1].y, o1);
    }
    const float inv = __builtin_amdgcn_rcpf(L);
    const u32 dst = g1h + (tbr * 8 + j) * (G1S * 8);
    const int pos = 12 * ((tp >> 2) & 7) + 4 * (tp >> 5) + (tp & 3);
    gstore2<AUX_DEV>(rG, dst + pos * 16, tg, o0 * inv, o1 * inv);
    if (tp == 0) { const float lse = M + __builtin_amdgcn_logf(L); gstore2<AUX_DEV>(rG, dst + DH * 8, tg, lse, lse); }
  }
  wo_ld(1);
  wo_ld(2);
  u32 ov;
  {
    const u32 rec = g1h + (brw * 8 + r) * (G1S * 8);
    const u32 src[3] = {rec + DH * 8, rec + (24 * w + 2 * c) * 8, c < 4 ? rec + (24 * w + 16 + 2 * c) * 8 : OOB};
    while (now() - t_pub < ((a.tau_b * PT1) >> 16)) __builtin_amdgcn_s_sleep(1);
    u32x4 g[3];
#pragma unroll
    for (int k = 0; k < 3; ++k) g[k] = gload2<AUX_DEV>(rG, src[k]);
    gpoll<AUX_DEV, 3>(rG, src, c < 4 ? ~0u : 0u, tg, g);
    const float lse = gv0(g[0]);
    const float f = __builtin_amdgcn_exp2f(lse - x16_max(fmaxf(lse, dpp<ROR8>(lse))));
    float v[5] = {f, f * gv0(g[1]), f * gv1(g[1]), f * gv0(g[2]), f * gv1(g[2])};
#pragma unroll
    for (int k = 0; k < 5; ++k) v[k] += dpp<ROR8>(v[k]);
#pragma unroll
    for (int k = 0; k < 5; ++k) v[k] = x16_sum(v[k]);
    const float inv = __builtin_amdgcn_rcpf(v[0]);
    ov = (lane & 8) ? pk_bf16(v[3] * inv, v[4] * inv) : pk_bf16(v[1] * inv, v[2] * inv);
  }
  {
    f32x4 y[4] = {f32x4{0.f, 0.f, 0.f, 0.f}, f32x4{0.f, 0.f, 0.f, 0.f}, f32x4{0.f, 0.f, 0.f, 0.f}, f32x4{0.f, 0.f, 0.f, 0.f}};
    const int sel = (((lane >> 3) & 1) * 32 + (lane >> 5)) * 4;
    const u32 psel = (lane & 16) ? 0x03020302u : 0x01000100u;
    u32 pr[6];
#pragma unroll
    for (int i = 0; i < 6; ++i) pr[i] = (u32)__builtin_amdgcn_ds_bpermute(sel + 8 * i, (int)ov);
    SB();
#pragma unroll
    for (int i = 0; i < 6; ++i) {
      const bf16x8 af = frag(__builtin_amdgcn_perm(pr[i], pr[i], psel));
      mma(y[2 * (i & 1)], af, wo[2 * i]); mma(y[2 * (i & 1) + 1], af, wo[2 * i + 1]);
    }
    if ((lane & 15) < 10) {
      float* yp = lds + L_Y + w * 320 + (lane >> 5) * 160 + (lane & 15) * 8 + ((lane >> 4) & 1) * 4;
      *(f32x4*)yp = y[0] + y[2]; *(f32x4*)(yp + 80) = y[1] + y[3];
    }
  }
  __syncthreads();
  if (tid < 160) {
    float2 u[8];
#pragma unroll
    for (int k = 0; k < 8; ++k) u[k] = *(const float2*)(lds + L_Y + k * 320 + 2 * tid);
    SB();
    const float y0 = ((u[0].x + u[1].x) + (u[2].x + u[3].x)) + ((u[4].x + u[5].x) + (u[6].x + u[7].x));
    const float y1 = ((u[0].y + u[1].y) + (u[2].y + u[3].y)) + ((u[4].y + u[5].y) + (u[6].y + u[7].y));
    const int bb = tid >= 80, p = tid - bb * 80;
    gstore2<AUX_DEV>(rG, WS_G2 + (((brw * NH + h) * 2 + bb) * DM + cq * 160 + 2 * p) * 8, tg, y0, y1);
  }
  if (w == 7 && wg < 160) {
    const int fbr = wg / 80, rem = wg - fbr * 80, fb2 = rem / 40, cb = rem - fb2 * 40;
    const u32 s0 = WS_G2 + (((fbr * NH + r) * 2 + fb2) * DM + cb * 16 + 2 * c) * 8;
    const u32 src[4] = {s0, s0 + 8 * 2 * DM * 8, s0 + 16 * 2 * DM * 8, s0 + 24 * 2 * DM * 8};
    __builtin_amdgcn_s_sleep(8);
    u32x4 g[4];
#pragma unroll
    for (int k = 0; k < 4; ++k) g[k] = gload2<AUX_DEV>(rG, src[k]);
    gpoll<AUX_DEV, 4>(rG, src, ~0u, tg, g);
    const float y0 = xoct_sum((gv0(g[0]) + gv0(g[1])) + (gv0(g[2]) + gv0(g[3])));
    const float y1 = xoct_sum((gv1(g[0]) + gv1(g[1])) + (gv1(g[2]) + gv1(g[3])));
    __builtin_amdgcn_raw_buffer_store_b32(pk_bf16(y0, y1), mk_rsrc(a.out, 2 * 2 * DM * 2),
                                          lane < 8 ? (fb2 * (2 * DM) + fbr * DM + cb * 16 + 2 * c) * 2 : OOB, 0, AUX_SYS);
    if (wg == 0 && lane == 0) __hip_atomic_store((gu64*)a.ws, tag, RLX_AGENT);
  }
}

extern "C" void kernel_launch(void* const* d_in, const int* in_sizes, int n_in, void* d_out, int out_size, void* d_ws,
                              size_t ws_size, hipStream_t stream) {
  Args a;
  for (int i = 0; i < 2; ++i) {
    a.x[i] = (const char*)d_in[5 * i + 0]; a.k[i] = (const char*)d_in[5 * i + 1]; a.v[i] = (const char*)d_in[5 * i + 2];
    a.wq[i] = (const char*)d_in[5 * i + 3]; a.wo[i] = (const char*)d_in[5 * i + 4];
  }
  a.out = (char*)d_out; a.ws = (char*)d_ws;
  int dev = 0, khz = 0;
  if (hipGetDevice(&dev) != hipSuccess || hipDeviceGetAttribute(&khz, hipDeviceAttributeWallClockRate, dev) != hipSuccess ||
      khz < 10000 || khz > 400000) khz = 100000;
  a.tau_a = (u32)((u64)TAU_A * (u64)khz / 100000u);
  a.tau_b = (u32)((u64)TAU_B * (u64)khz / 100000u);
  hipLaunchKernelGGL(fused_decode, dim3(NWG), dim3(NTHR), 0, stream, a);
}
```
